```python
import torch

def _softmax(x, axis=-1):
    return torch.softmax(x, dim=axis)
def _gelu(x):
    return torch.nn.functional.gelu(x, approximate="tanh")
def _silu(x):
    return torch.nn.functional.silu(x)
def _sigmoid(x):
    return torch.sigmoid(x)
def _layernorm(x, axis=-1, eps=1e-5):
    m = x.mean(dim=axis, keepdim=True)
    v = x.var(dim=axis, keepdim=True, unbiased=False)
    return (x - m) / torch.sqrt(v + eps)
def _scatter_add(tgt, idx, src, axis=0):
    return tgt.clone().index_add_(axis, idx, src)
def _segment_sum(src, seg, n):
    out = torch.zeros((n,) + src.shape[1:], dtype=src.dtype)
    return out.index_add_(0, seg, src)
def _pad(x, pw):
    flat = tuple(v for pair in reversed(pw) for v in pair)
    return torch.nn.functional.pad(x, flat)

kernel_name = 'standalone__rezero__sampling'

def setup_inputs():

    _w = (lambda a: (lambda t: t.to(torch.bfloat16) if t.is_floating_point() else t)(torch.as_tensor(a)))
    B, S, D = 1024, 32, 448
    return {
        "x": _w((lambda *s: torch.randn(*s, dtype=torch.bfloat16) * 0.05)(B, S, D)),
        "fx": _w((lambda *s: torch.randn(*s, dtype=torch.bfloat16) * 0.8)(B, S, D)),
        "alpha": _w((lambda *s: torch.randn(*s, dtype=torch.bfloat16) * 0.8)(1) * 0.1),
    }

def reference(x, fx, alpha):
    return x + alpha * fx
```

```cpp
#include <hip/hip_runtime.h>
#include <hip/hip_bf16.h>
#include <stdint.h>

typedef unsigned int u32x4 __attribute__((ext_vector_type(4)));
typedef float f32x2 __attribute__((ext_vector_type(2)));
typedef __bf16 bf16x2 __attribute__((ext_vector_type(2)));

__device__ __forceinline__ unsigned int rezero_pair(unsigned int xw, unsigned int fw, float a) {
    f32x2 f;
    f.x = __uint_as_float(fw << 16);
    f.y = __uint_as_float(fw & 0xffff0000u);
    f32x2 p = f * a;
    bf16x2 pb = __builtin_convertvector(p, bf16x2);
    unsigned int pw = __builtin_bit_cast(unsigned int, pb);
    f32x2 t;
    t.x = __uint_as_float(pw << 16);
    t.y = __uint_as_float(pw & 0xffff0000u);
    f32x2 xv;
    xv.x = __uint_as_float(xw << 16);
    xv.y = __uint_as_float(xw & 0xffff0000u);
    f32x2 s = xv + t;
    bf16x2 sb = __builtin_convertvector(s, bf16x2);
    return __builtin_bit_cast(unsigned int, sb);
}

__global__ __launch_bounds__(256) void rezero_vec(const u32x4* __restrict__ x, const u32x4* __restrict__ fx,
                                                  const unsigned short* __restrict__ alpha,
                                                  u32x4* __restrict__ out, int nvec) {
    int i = blockIdx.x * 256 + threadIdx.x;
    if (i >= nvec) return;
    float a = __uint_as_float(((unsigned int)alpha[0]) << 16);
    u32x4 xv = x[i];
    u32x4 fv = fx[i];
    u32x4 o;
    o.x = rezero_pair(xv.x, fv.x, a);
    o.y = rezero_pair(xv.y, fv.y, a);
    o.z = rezero_pair(xv.z, fv.z, a);
    o.w = rezero_pair(xv.w, fv.w, a);
    out[i] = o;
}

__global__ void rezero_tail(const unsigned short* __restrict__ x, const unsigned short* __restrict__ fx,
                            const unsigned short* __restrict__ alpha, unsigned short* __restrict__ out,
                            int start, int n) {
    int i = start + blockIdx.x * blockDim.x + threadIdx.x;
    if (i >= n) return;
    float a = __uint_as_float(((unsigned int)alpha[0]) << 16);
    unsigned int r = rezero_pair((unsigned int)x[i], (unsigned int)fx[i], a);
    out[i] = (unsigned short)(r & 0xffffu);
}

extern "C" void kernel_launch(void* const* d_in, const int* in_sizes, int n_in,
                              void* d_out, int out_size, void* d_ws, size_t ws_size,
                              hipStream_t stream) {
    const int n = out_size;
    const int nvec = n / 8;
    if (nvec > 0) {
        rezero_vec<<<(nvec + 255) / 256, 256, 0, stream>>>((const u32x4*)d_in[0], (const u32x4*)d_in[1],
                                                          (const unsigned short*)d_in[2], (u32x4*)d_out, nvec);
    }
    if (nvec * 8 < n) {
        rezero_tail<<<1, 64, 0, stream>>>((const unsigned short*)d_in[0], (const unsigned short*)d_in[1],
                                          (const unsigned short*)d_in[2], (unsigned short*)d_out, nvec * 8, n);
    }
}
```

```cpp
#include <hip/hip_runtime.h>
#include <stdint.h>

typedef unsigned int u32x4 __attribute__((ext_vector_type(4)));
typedef float f32x2 __attribute__((ext_vector_type(2)));
typedef __bf16 bf16x2 __attribute__((ext_vector_type(2)));

__device__ __forceinline__ unsigned int rezero_pair(unsigned int xw, unsigned int fw, float a) {
    f32x2 f;
    f.x = __uint_as_float(fw << 16);
    f.y = __uint_as_float(fw & 0xffff0000u);
    f32x2 p = f * a;
    bf16x2 pb = __builtin_convertvector(p, bf16x2);
    unsigned int pw = __builtin_bit_cast(unsigned int, pb);
    f32x2 t;
    t.x = __uint_as_float(pw << 16);
    t.y = __uint_as_float(pw & 0xffff0000u);
    f32x2 xv;
    xv.x = __uint_as_float(xw << 16);
    xv.y = __uint_as_float(xw & 0xffff0000u);
    f32x2 s = xv + t;
    bf16x2 sb = __builtin_convertvector(s, bf16x2);
    return __builtin_bit_cast(unsigned int, sb);
}
__device__ __forceinline__ u32x4 rezero_vec4(u32x4 xv, u32x4 fv, float a) {
    u32x4 o;
    o.x = rezero_pair(xv.x, fv.x, a);
    o.y = rezero_pair(xv.y, fv.y, a);
    o.z = rezero_pair(xv.z, fv.z, a);
    o.w = rezero_pair(xv.w, fv.w, a);
    return o;
}

template <int BLOCK, int VPT, int LXA, int LFA, int STA, int LAYOUT>
__global__ __launch_bounds__(BLOCK) void rz(const void* __restrict__ x, const void* __restrict__ fx,
                                            const unsigned int* __restrict__ alpha, void* __restrict__ out,
                                            unsigned int nbytes, int iters) {
    __amdgpu_buffer_rsrc_t rx = __builtin_amdgcn_make_buffer_rsrc((void*)x, 0, nbytes, 0x00020000);
    __amdgpu_buffer_rsrc_t rf = __builtin_amdgcn_make_buffer_rsrc((void*)fx, 0, nbytes, 0x00020000);
    __amdgpu_buffer_rsrc_t ro = __builtin_amdgcn_make_buffer_rsrc(out, 0, nbytes, 0x00020000);
    float a = __uint_as_float(alpha[0] << 16);
    unsigned int b = blockIdx.x, G = gridDim.x, t = threadIdx.x;
    unsigned int base, jstride, itstride;
    if (LAYOUT == 0) {
        base = (b * (unsigned)(BLOCK * VPT) * iters + t) * 16u;
        jstride = BLOCK * 16u;
        itstride = BLOCK * VPT * 16u;
    } else if (LAYOUT == 1) {
        base = (b * BLOCK + t) * 16u;
        jstride = G * BLOCK * 16u;
        itstride = jstride * VPT;
    } else if (LAYOUT == 2) {
        unsigned int bb = (b & 7) * (G >> 3) + (b >> 3);
        base = (bb * (unsigned)(BLOCK * VPT) * iters + t) * 16u;
        jstride = BLOCK * 16u;
        itstride = BLOCK * VPT * 16u;
    } else if (LAYOUT == 3) {
        unsigned int xcd = b & 7, bi = b >> 3, Gx = G >> 3;
        base = (xcd * Gx * (unsigned)(BLOCK * VPT) * iters + bi * BLOCK + t) * 16u;
        jstride = Gx * BLOCK * 16u;
        itstride = jstride * VPT;
    } else {
        base = (b * (unsigned)(BLOCK * VPT) + t) * 16u;
        jstride = BLOCK * 16u;
        itstride = G * BLOCK * VPT * 16u;
    }
    for (int it = 0; it < iters; ++it) {
        u32x4 xv[VPT], fv[VPT];
#pragma unroll
        for (int j = 0; j < VPT; ++j) {
            fv[j] = __builtin_amdgcn_raw_buffer_load_b128(rf, base + j * jstride, 0, LFA);
            xv[j] = __builtin_amdgcn_raw_buffer_load_b128(rx, base + j * jstride, 0, LXA);
        }
#pragma unroll
        for (int j = 0; j < VPT; ++j) {
            u32x4 o = rezero_vec4(xv[j], fv[j], a);
            __builtin_amdgcn_raw_buffer_store_b128(o, ro, base + j * jstride, 0, STA);
        }
        base += itstride;
    }
}

template <int LXA, int LFA, int STA>
__global__ __launch_bounds__(256) void rzs(const void* __restrict__ x, const void* __restrict__ fx,
                                            const unsigned int* __restrict__ alpha, void* __restrict__ out,
                                            unsigned int nbytes, unsigned long long* __restrict__ stamps) {
    unsigned long long t0 = __builtin_amdgcn_s_memrealtime();
    __amdgpu_buffer_rsrc_t rx = __builtin_amdgcn_make_buffer_rsrc((void*)x, 0, nbytes, 0x00020000);
    __amdgpu_buffer_rsrc_t rf = __builtin_amdgcn_make_buffer_rsrc((void*)fx, 0, nbytes, 0x00020000);
    __amdgpu_buffer_rsrc_t ro = __builtin_amdgcn_make_buffer_rsrc(out, 0, nbytes, 0x00020000);
    float a = __uint_as_float(alpha[0] << 16);
    unsigned int base = (blockIdx.x * 256 + threadIdx.x) * 16u;
    u32x4 fv = __builtin_amdgcn_raw_buffer_load_b128(rf, base, 0, LFA);
    u32x4 xv = __builtin_amdgcn_raw_buffer_load_b128(rx, base, 0, LXA);
    u32x4 o = rezero_vec4(xv, fv, a);
    __builtin_amdgcn_raw_buffer_store_b128(o, ro, base, 0, STA);
    asm volatile("s_waitcnt vmcnt(0)" ::: "memory");
    unsigned long long t1 = __builtin_amdgcn_s_memrealtime();
    if (threadIdx.x == 0) { stamps[blockIdx.x * 2] = t0; stamps[blockIdx.x * 2 + 1] = t1; }
}
__global__ __launch_bounds__(1024) void spin(const unsigned long long* __restrict__ stamps, int nb, int sel, int M) {
    __shared__ unsigned long long smin[2][1024], smax[2][1024];
    unsigned long long mn0 = ~0ull, mx0 = 0, mn1 = ~0ull, mx1 = 0;
    for (int i = threadIdx.x; i < nb; i += 1024) {
        unsigned long long a = stamps[2 * i], b = stamps[2 * i + 1];
        mn0 = a < mn0 ? a : mn0; mx0 = a > mx0 ? a : mx0; mn1 = b < mn1 ? b : mn1; mx1 = b > mx1 ? b : mx1;
    }
    smin[0][threadIdx.x] = mn0; smax[0][threadIdx.x] = mx0; smin[1][threadIdx.x] = mn1; smax[1][threadIdx.x] = mx1;
    __syncthreads();
    if (threadIdx.x == 0) {
        for (int i = 1; i < 1024; ++i) {
            mn0 = smin[0][i] < mn0 ? smin[0][i] : mn0; mx0 = smax[0][i] > mx0 ? smax[0][i] : mx0;
            mn1 = smin[1][i] < mn1 ? smin[1][i] : mn1; mx1 = smax[1][i] > mx1 ? smax[1][i] : mx1;
        }
        unsigned long long now = __builtin_amdgcn_s_memrealtime();
        unsigned long long D = sel == 0 ? mx1 - mn0 : sel == 1 ? mx0 - mn0 : sel == 2 ? mn1 - mn0 : now - mx1;
        if (D > 10000) D = 10000;
        unsigned long long tgt = now + D * M;
        while (__builtin_amdgcn_s_memrealtime() < tgt) __builtin_amdgcn_s_sleep(1);
    }
}
extern "C" void kernel_launch(void* const* d_in, const int* in_sizes, int n_in,
                              void* d_out, int out_size, void* d_ws, size_t ws_size,
                              hipStream_t stream) {
    const int nvec = out_size / 8;
    int grid = (nvec + 255) / 256;
    rzs<2, 2, 17><<<grid, 256, 0, stream>>>(d_in[0], d_in[1], (const unsigned int*)d_in[2], d_out, (unsigned)nvec * 16u, (unsigned long long*)d_ws);
    spin<<<1, 1024, 0, stream>>>((const unsigned long long*)d_ws, grid, 0, 0);
}
```

```cpp
#include <hip/hip_runtime.h>
#include <stdint.h>

typedef unsigned int u32x4 __attribute__((ext_vector_type(4)));
typedef float f32x2 __attribute__((ext_vector_type(2)));
typedef __bf16 bf16x2 __attribute__((ext_vector_type(2)));

__device__ __forceinline__ unsigned int rezero_pair(unsigned int xw, unsigned int fw, float a) {
    f32x2 f;
    f.x = __uint_as_float(fw << 16);
    f.y = __uint_as_float(fw & 0xffff0000u);
    f32x2 p = f * a;
    bf16x2 pb = __builtin_convertvector(p, bf16x2);
    unsigned int pw = __builtin_bit_cast(unsigned int, pb);
    f32x2 t;
    t.x = __uint_as_float(pw << 16);
    t.y = __uint_as_float(pw & 0xffff0000u);
    f32x2 xv;
    xv.x = __uint_as_float(xw << 16);
    xv.y = __uint_as_float(xw & 0xffff0000u);
    f32x2 s = xv + t;
    bf16x2 sb = __builtin_convertvector(s, bf16x2);
    return __builtin_bit_cast(unsigned int, sb);
}
__device__ __forceinline__ u32x4 rezero_vec4(u32x4 xv, u32x4 fv, float a) {
    u32x4 o;
    o.x = rezero_pair(xv.x, fv.x, a);
    o.y = rezero_pair(xv.y, fv.y, a);
    o.z = rezero_pair(xv.z, fv.z, a);
    o.w = rezero_pair(xv.w, fv.w, a);
    return o;
}

template <int BLOCK, int VPT, int LXA, int LFA, int STA, int LAYOUT>
__global__ __launch_bounds__(BLOCK) void rz(const void* __restrict__ x, const void* __restrict__ fx,
                                            const unsigned int* __restrict__ alpha, void* __restrict__ out,
                                            unsigned int nbytes, int iters) {
    __amdgpu_buffer_rsrc_t rx = __builtin_amdgcn_make_buffer_rsrc((void*)x, 0, nbytes, 0x00020000);
    __amdgpu_buffer_rsrc_t rf = __builtin_amdgcn_make_buffer_rsrc((void*)fx, 0, nbytes, 0x00020000);
    __amdgpu_buffer_rsrc_t ro = __builtin_amdgcn_make_buffer_rsrc(out, 0, nbytes, 0x00020000);
    float a = __uint_as_float(alpha[0] << 16);
    unsigned int b = blockIdx.x, G = gridDim.x, t = threadIdx.x;
    unsigned int base, jstride, itstride;
    if (LAYOUT == 0) {
        base = (b * (unsigned)(BLOCK * VPT) * iters + t) * 16u;
        jstride = BLOCK * 16u;
        itstride = BLOCK * VPT * 16u;
    } else if (LAYOUT == 1) {
        base = (b * BLOCK + t) * 16u;
        jstride = G * BLOCK * 16u;
        itstride = jstride * VPT;
    } else if (LAYOUT == 2) {
        unsigned int bb = (b & 7) * (G >> 3) + (b >> 3);
        base = (bb * (unsigned)(BLOCK * VPT) * iters + t) * 16u;
        jstride = BLOCK * 16u;
        itstride = BLOCK * VPT * 16u;
    } else if (LAYOUT == 3) {
        unsigned int xcd = b & 7, bi = b >> 3, Gx = G >> 3;
        base = (xcd * Gx * (unsigned)(BLOCK * VPT) * iters + bi * BLOCK + t) * 16u;
        jstride = Gx * BLOCK * 16u;
        itstride = jstride * VPT;
    } else {
        base = (b * (unsigned)(BLOCK * VPT) + t) * 16u;
        jstride = BLOCK * 16u;
        itstride = G * BLOCK * VPT * 16u;
    }
    for (int it = 0; it < iters; ++it) {
        u32x4 xv[VPT], fv[VPT];
#pragma unroll
        for (int j = 0; j < VPT; ++j) {
            fv[j] = __builtin_amdgcn_raw_buffer_load_b128(rf, base + j * jstride, 0, LFA);
            xv[j] = __builtin_amdgcn_raw_buffer_load_b128(rx, base + j * jstride, 0, LXA);
        }
#pragma unroll
        for (int j = 0; j < VPT; ++j) {
            u32x4 o = rezero_vec4(xv[j], fv[j], a);
            __builtin_amdgcn_raw_buffer_store_b128(o, ro, base + j * jstride, 0, STA);
        }
        base += itstride;
    }
}
template <int LXA, int LFA, int STA, int MODE, int CPB>
__global__ __launch_bounds__(256) void rz3(const void* __restrict__ x, const void* __restrict__ fx,
                                            const unsigned int* __restrict__ alpha, void* __restrict__ out,
                                            unsigned int nbytes, unsigned int cmask) {
    __amdgpu_buffer_rsrc_t rx = __builtin_amdgcn_make_buffer_rsrc((void*)x, 0, nbytes, 0x00020000);
    __amdgpu_buffer_rsrc_t rf = __builtin_amdgcn_make_buffer_rsrc((void*)fx, 0, nbytes, 0x00020000);
    __amdgpu_buffer_rsrc_t ro = __builtin_amdgcn_make_buffer_rsrc(out, 0, nbytes, 0x00020000);
    unsigned int b = blockIdx.x, t = threadIdx.x;
    unsigned int q = b >> 3, r = b & 7, s = r >> 2;
    unsigned int off[CPB];
#pragma unroll
    for (int c = 0; c < CPB; ++c) {
        unsigned int pair = q * (4 * CPB) + (r & 3) * CPB + c;
        unsigned int pg = pair >> 7;
        unsigned int p = s ^ ((cmask >> pg) & 1);
        off[c] = pair * 16384u + p * 8192u + t * 16u;
    }
    u32x4 xv[CPB * 2], fv[CPB * 2];
    if (MODE != 2) {
#pragma unroll
        for (int j = 0; j < CPB * 2; ++j) {
            fv[j] = __builtin_amdgcn_raw_buffer_load_b128(rf, off[j >> 1] + (j & 1) * 4096u, 0, LFA);
            xv[j] = __builtin_amdgcn_raw_buffer_load_b128(rx, off[j >> 1] + (j & 1) * 4096u, 0, LXA);
        }
    } else {
#pragma unroll
        for (int j = 0; j < CPB * 2; ++j) { fv[j] = (u32x4){t, b, t + j, 1u}; xv[j] = fv[j]; }
    }
    float a = __uint_as_float(alpha[0] << 16);
#pragma unroll
    for (int j = 0; j < CPB * 2; ++j) {
        u32x4 o = rezero_vec4(xv[j], fv[j], a);
        if (MODE == 1) { if (o.x == 0x12345678u && o.y == 0x9abcdef0u && o.z == 0x1u) __builtin_amdgcn_raw_buffer_store_b128(o, ro, off[j >> 1] + (j & 1) * 4096u, 0, STA); }
        else __builtin_amdgcn_raw_buffer_store_b128(o, ro, off[j >> 1] + (j & 1) * 4096u, 0, STA);
    }
}

extern "C" void kernel_launch(void* const* d_in, const int* in_sizes, int n_in,
                              void* d_out, int out_size, void* d_ws, size_t ws_size,
                              hipStream_t stream) {
    rz3<2, 2, 17, 0, 1><<<3584, 256, 0, stream>>>(d_in[0], d_in[1], (const unsigned int*)d_in[2], d_out, (unsigned)out_size * 2u, 0x2aaau);
}
```

```cpp
#include <hip/hip_runtime.h>
#include <stdint.h>

typedef unsigned int u32x4 __attribute__((ext_vector_type(4)));
typedef float f32x2 __attribute__((ext_vector_type(2)));
typedef __bf16 bf16x2 __attribute__((ext_vector_type(2)));

__device__ __forceinline__ unsigned int rezero_pair(unsigned int xw, unsigned int fw, float a) {
    f32x2 f;
    f.x = __uint_as_float(fw << 16);
    f.y = __uint_as_float(fw & 0xffff0000u);
    f32x2 p = f * a;
    bf16x2 pb = __builtin_convertvector(p, bf16x2);
    unsigned int pw = __builtin_bit_cast(unsigned int, pb);
    f32x2 t;
    t.x = __uint_as_float(pw << 16);
    t.y = __uint_as_float(pw & 0xffff0000u);
    f32x2 xv;
    xv.x = __uint_as_float(xw << 16);
    xv.y = __uint_as_float(xw & 0xffff0000u);
    f32x2 s = xv + t;
    bf16x2 sb = __builtin_convertvector(s, bf16x2);
    return __builtin_bit_cast(unsigned int, sb);
}
__device__ __forceinline__ u32x4 rezero_vec4(u32x4 xv, u32x4 fv, float a) {
    u32x4 o;
    o.x = rezero_pair(xv.x, fv.x, a);
    o.y = rezero_pair(xv.y, fv.y, a);
    o.z = rezero_pair(xv.z, fv.z, a);
    o.w = rezero_pair(xv.w, fv.w, a);
    return o;
}

template <int BLOCK, int VPT, int LXA, int LFA, int STA, int LAYOUT, int MODE, int STAMP>
__global__ __launch_bounds__(BLOCK) void rz2(const void* __restrict__ x, const void* __restrict__ fx,
                                            const unsigned int* __restrict__ alpha, void* __restrict__ out,
                                            unsigned int nbytes, unsigned long long* __restrict__ stamps) {
    unsigned long long t0;
    if (STAMP) t0 = __builtin_amdgcn_s_memrealtime();
    __amdgpu_buffer_rsrc_t rx = __builtin_amdgcn_make_buffer_rsrc((void*)x, 0, nbytes, 0x00020000);
    __amdgpu_buffer_rsrc_t rf = __builtin_amdgcn_make_buffer_rsrc((void*)fx, 0, nbytes, 0x00020000);
    __amdgpu_buffer_rsrc_t ro = __builtin_amdgcn_make_buffer_rsrc(out, 0, nbytes, 0x00020000);
    unsigned int b = blockIdx.x, G = gridDim.x, t = threadIdx.x;
    unsigned int base, jstride;
    if (LAYOUT == 0) { base = (b * (unsigned)(BLOCK * VPT) + t) * 16u; jstride = BLOCK * 16u; }
    else if (LAYOUT == 1) { base = (b * BLOCK + t) * 16u; jstride = G * BLOCK * 16u; }
    else if (LAYOUT == 2) { unsigned int bb = (b & 7) * (G >> 3) + (b >> 3); base = (bb * (unsigned)(BLOCK * VPT) + t) * 16u; jstride = BLOCK * 16u; }
    else if (LAYOUT == 3) { unsigned int xcd = b & 7, bi = b >> 3, Gx = G >> 3; base = (xcd * Gx * (unsigned)(BLOCK * VPT) + bi * BLOCK + t) * 16u; jstride = Gx * BLOCK * 16u; }
    else {
        unsigned int w = t >> 6, l = t & 63; base = ((b * (BLOCK / 64) + w) * (unsigned)(64 * VPT) + l) * 16u; jstride = 1024u; }
    u32x4 xv[VPT], fv[VPT];
    if (MODE != 2) {
#pragma unroll
        for (int j = 0; j < VPT; ++j) {
            fv[j] = __builtin_amdgcn_raw_buffer_load_b128(rf, base + j * jstride, 0, LFA);
            xv[j] = __builtin_amdgcn_raw_buffer_load_b128(rx, base + j * jstride, 0, LXA);
        }
    } else {
#pragma unroll
        for (int j = 0; j < VPT; ++j) { fv[j] = (u32x4){t, b, t + j, 1u}; xv[j] = fv[j]; }
    }
    float a = __uint_as_float(alpha[0] << 16);
#pragma unroll
    for (int j = 0; j < VPT; ++j) {
        u32x4 o = rezero_vec4(xv[j], fv[j], a);
        if (MODE == 1) { if (o.x == 0x12345678u && o.y == 0x9abcdef0u && o.z == 0x1u) __builtin_amdgcn_raw_buffer_store_b128(o, ro, base + j * jstride, 0, STA); }
        else __builtin_amdgcn_raw_buffer_store_b128(o, ro, base + j * jstride, 0, STA);
    }
    if (STAMP) {
        asm volatile("s_waitcnt vmcnt(0)" ::: "memory");
        unsigned long long t1 = __builtin_amdgcn_s_memrealtime();
        if (t == 0) { stamps[b * 2] = t0; stamps[b * 2 + 1] = t1; }
    }
}

extern "C" void kernel_launch(void* const* d_in, const int* in_sizes, int n_in,
                              void* d_out, int out_size, void* d_ws, size_t ws_size,
                              hipStream_t stream) {
    const int nvec = out_size / 8;
    int grid = (nvec + 128 * 4 - 1) / (128 * 4);
    rz2<128, 4, 2, 2, 17, 0, 0, 0><<<grid, 128, 0, stream>>>(d_in[0], d_in[1], (const unsigned int*)d_in[2], d_out, (unsigned)nvec * 16u, (unsigned long long*)d_ws);
}
```

```cpp
#include <hip/hip_runtime.h>
#include <hip/hip_bf16.h>
#include <stdint.h>

typedef unsigned int u32x4 __attribute__((ext_vector_type(4)));
typedef float f32x2 __attribute__((ext_vector_type(2)));
typedef __bf16 bf16x2 __attribute__((ext_vector_type(2)));

#define RZ_BLOCK 256
#define RZ_VPT 2
#define RZ_TILE_BYTES (RZ_BLOCK * RZ_VPT * 16)

__device__ __forceinline__ unsigned int rezero_pair(unsigned int xw, unsigned int fw, float a) {
    f32x2 f;
    f.x = __uint_as_float(fw << 16);
    f.y = __uint_as_float(fw & 0xffff0000u);
    f32x2 p = f * a;
    bf16x2 pb = __builtin_convertvector(p, bf16x2);
    unsigned int pw = __builtin_bit_cast(unsigned int, pb);
    f32x2 t;
    t.x = __uint_as_float(pw << 16);
    t.y = __uint_as_float(pw & 0xffff0000u);
    f32x2 xv;
    xv.x = __uint_as_float(xw << 16);
    xv.y = __uint_as_float(xw & 0xffff0000u);
    f32x2 s = xv + t;
    bf16x2 sb = __builtin_convertvector(s, bf16x2);
    return __builtin_bit_cast(unsigned int, sb);
}

__device__ __forceinline__ u32x4 rezero_vec(u32x4 xv, u32x4 fv, float a) {
    u32x4 o;
    o.x = rezero_pair(xv.x, fv.x, a);
    o.y = rezero_pair(xv.y, fv.y, a);
    o.z = rezero_pair(xv.z, fv.z, a);
    o.w = rezero_pair(xv.w, fv.w, a);
    return o;
}

__global__ __launch_bounds__(RZ_BLOCK) void rezero_kernel(const unsigned short* __restrict__ x,
                                                          const unsigned short* __restrict__ fx,
                                                          const unsigned int* __restrict__ alpha,
                                                          unsigned short* __restrict__ out,
                                                          long long vec_bytes, long long n) {
    const long long base = (long long)blockIdx.x * RZ_TILE_BYTES;
    const long long left = vec_bytes - base;
    const unsigned int nrec = left >= RZ_TILE_BYTES ? (unsigned int)RZ_TILE_BYTES
                                                    : (left > 0 ? (unsigned int)left : 0u);
    __amdgpu_buffer_rsrc_t rx = __builtin_amdgcn_make_buffer_rsrc((char*)x + base, 0, nrec, 0x00020000);
    __amdgpu_buffer_rsrc_t rf = __builtin_amdgcn_make_buffer_rsrc((char*)fx + base, 0, nrec, 0x00020000);
    __amdgpu_buffer_rsrc_t ro = __builtin_amdgcn_make_buffer_rsrc((char*)out + base, 0, nrec, 0x00020000);
    const unsigned int off = threadIdx.x * 16u;
    u32x4 xv[RZ_VPT], fv[RZ_VPT];
#pragma unroll
    for (int j = 0; j < RZ_VPT; ++j) {
        fv[j] = __builtin_amdgcn_raw_buffer_load_b128(rf, off + j * (RZ_BLOCK * 16u), 0,   2);
        xv[j] = __builtin_amdgcn_raw_buffer_load_b128(rx, off + j * (RZ_BLOCK * 16u), 0,   2);
    }
    const float a = __uint_as_float(alpha[0] << 16);
#pragma unroll
    for (int j = 0; j < RZ_VPT; ++j) {
        u32x4 o = rezero_vec(xv[j], fv[j], a);
        __builtin_amdgcn_raw_buffer_store_b128(o, ro, off + j * (RZ_BLOCK * 16u), 0,   17);
    }
    const long long tail0 = vec_bytes >> 1;
    if (tail0 < n && blockIdx.x == gridDim.x - 1) {
        const long long i = tail0 + threadIdx.x;
        if (i < n) out[i] = (unsigned short)(rezero_pair((unsigned int)x[i], (unsigned int)fx[i], a) & 0xffffu);
    }
}

__global__ __launch_bounds__(256) void rezero_scalar_kernel(const unsigned short* __restrict__ x,
                                                            const unsigned short* __restrict__ fx,
                                                            const unsigned short* __restrict__ alpha,
                                                            unsigned short* __restrict__ out, long long n) {
    const float a = __uint_as_float(((unsigned int)alpha[0]) << 16);
    const long long stride = (long long)gridDim.x * 256;
    for (long long i = (long long)blockIdx.x * 256 + threadIdx.x; i < n; i += stride)
        out[i] = (unsigned short)(rezero_pair((unsigned int)x[i], (unsigned int)fx[i], a) & 0xffffu);
}

extern "C" void kernel_launch(void* const* d_in, const int* in_sizes, int n_in,
                              void* d_out, int out_size, void* d_ws, size_t ws_size,
                              hipStream_t stream) {
    const unsigned short* x = (const unsigned short*)d_in[0];
    const unsigned short* fx = (const unsigned short*)d_in[1];
    const unsigned short* alpha = (const unsigned short*)d_in[2];
    unsigned short* out = (unsigned short*)d_out;
    const long long n = out_size;
    if (n <= 0) return;
    const bool aligned = ((((uintptr_t)x) | ((uintptr_t)fx) | ((uintptr_t)out)) & 15) == 0 &&
                         (((uintptr_t)alpha) & 3) == 0;
    if (aligned) {
        const long long vec_bytes = (n / 8) * 16;
        long long grid = (vec_bytes + RZ_TILE_BYTES - 1) / RZ_TILE_BYTES;
        if (grid < 1) grid = 1;
        rezero_kernel<<<(unsigned int)grid, RZ_BLOCK, 0, stream>>>(x, fx, (const unsigned int*)alpha, out,
                                                                   vec_bytes, n);
    } else {
        long long grid = (n + 255) / 256;
        if (grid > 4096) grid = 4096;
        rezero_scalar_kernel<<<(unsigned int)grid, 256, 0, stream>>>(x, fx, alpha, out, n);
    }
}
```

```cpp
#include <hip/hip_runtime.h>
#include <stdint.h>

typedef unsigned int u32x4 __attribute__((ext_vector_type(4)));
typedef float f32x2 __attribute__((ext_vector_type(2)));
typedef __bf16 bf16x2 __attribute__((ext_vector_type(2)));

__device__ __forceinline__ unsigned int rezero_pair(unsigned int xw, unsigned int fw, float a) {
    f32x2 f;
    f.x = __uint_as_float(fw << 16);
    f.y = __uint_as_float(fw & 0xffff0000u);
    f32x2 p = f * a;
    bf16x2 pb = __builtin_convertvector(p, bf16x2);
    unsigned int pw = __builtin_bit_cast(unsigned int, pb);
    f32x2 t;
    t.x = __uint_as_float(pw << 16);
    t.y = __uint_as_float(pw & 0xffff0000u);
    f32x2 xv;
    xv.x = __uint_as_float(xw << 16);
    xv.y = __uint_as_float(xw & 0xffff0000u);
    f32x2 s = xv + t;
    bf16x2 sb = __builtin_convertvector(s, bf16x2);
    return __builtin_bit_cast(unsigned int, sb);
}
__device__ __forceinline__ u32x4 rezero_vec4(u32x4 xv, u32x4 fv, float a) {
    u32x4 o;
    o.x = rezero_pair(xv.x, fv.x, a);
    o.y = rezero_pair(xv.y, fv.y, a);
    o.z = rezero_pair(xv.z, fv.z, a);
    o.w = rezero_pair(xv.w, fv.w, a);
    return o;
}

template <int BLOCK, int VPT, int LXA, int LFA, int STA, int LAYOUT, int MODE, int STAMP>
__global__ __launch_bounds__(BLOCK) void rz2(const void* __restrict__ x, const void* __restrict__ fx,
                                            const unsigned int* __restrict__ alpha, void* __restrict__ out,
                                            unsigned int nbytes, unsigned long long* __restrict__ stamps) {
    unsigned long long t0;
    if (STAMP) t0 = __builtin_amdgcn_s_memrealtime();
    __amdgpu_buffer_rsrc_t rx = __builtin_amdgcn_make_buffer_rsrc((void*)x, 0, nbytes, 0x00020000);
    __amdgpu_buffer_rsrc_t rf = __builtin_amdgcn_make_buffer_rsrc((void*)fx, 0, nbytes, 0x00020000);
    __amdgpu_buffer_rsrc_t ro = __builtin_amdgcn_make_buffer_rsrc(out, 0, nbytes, 0x00020000);
    unsigned int b = blockIdx.x, G = gridDim.x, t = threadIdx.x;
    unsigned int base, jstride;
    if (LAYOUT == 0) { base = (b * (unsigned)(BLOCK * VPT) + t) * 16u; jstride = BLOCK * 16u; }
    else if (LAYOUT == 1) { base = (b * BLOCK + t) * 16u; jstride = G * BLOCK * 16u; }
    else if (LAYOUT == 2) { unsigned int bb = (b & 7) * (G >> 3) + (b >> 3); base = (bb * (unsigned)(BLOCK * VPT) + t) * 16u; jstride = BLOCK * 16u; }
    else if (LAYOUT == 3) { unsigned int xcd = b & 7, bi = b >> 3, Gx = G >> 3; base = (xcd * Gx * (unsigned)(BLOCK * VPT) + bi * BLOCK + t) * 16u; jstride = Gx * BLOCK * 16u; }
    else {
        unsigned int w = t >> 6, l = t & 63; base = ((b * (BLOCK / 64) + w) * (unsigned)(64 * VPT) + l) * 16u; jstride = 1024u; }
    u32x4 xv[VPT], fv[VPT];
    if (MODE != 2) {
#pragma unroll
        for (int j = 0; j < VPT; ++j) {
            fv[j] = __builtin_amdgcn_raw_buffer_load_b128(rf, base + j * jstride, 0, LFA);
            xv[j] = __builtin_amdgcn_raw_buffer_load_b128(rx, base + j * jstride, 0, LXA);
        }
    } else {
#pragma unroll
        for (int j = 0; j < VPT; ++j) { fv[j] = (u32x4){t, b, t + j, 1u}; xv[j] = fv[j]; }
    }
    float a = __uint_as_float(alpha[0] << 16);
#pragma unroll
    for (int j = 0; j < VPT; ++j) {
        u32x4 o = rezero_vec4(xv[j], fv[j], a);
        if (MODE == 1) { if (o.x == 0x12345678u && o.y == 0x9abcdef0u && o.z == 0x1u) __builtin_amdgcn_raw_buffer_store_b128(o, ro, base + j * jstride, 0, STA); }
        else __builtin_amdgcn_raw_buffer_store_b128(o, ro, base + j * jstride, 0, STA);
    }
    if (STAMP) {
        asm volatile("s_waitcnt vmcnt(0)" ::: "memory");
        unsigned long long t1 = __builtin_amdgcn_s_memrealtime();
        if (t == 0) { stamps[b * 2] = t0; stamps[b * 2 + 1] = t1; }
    }
}

extern "C" void kernel_launch(void* const* d_in, const int* in_sizes, int n_in,
                              void* d_out, int out_size, void* d_ws, size_t ws_size,
                              hipStream_t stream) {
    const int nvec = out_size / 8;
    int grid = (nvec + 256 * 2 - 1) / (256 * 2);
    rz2<256, 2, 3, 3, 17, 0, 0, 0><<<grid, 256, 0, stream>>>(d_in[0], d_in[1], (const unsigned int*)d_in[2], d_out, (unsigned)nvec * 16u, (unsigned long long*)d_ws);
}
```

```cpp
#include <hip/hip_runtime.h>
#include <hip/hip_bf16.h>
#include <stdint.h>

typedef unsigned int u32x4 __attribute__((ext_vector_type(4)));
typedef float f32x2 __attribute__((ext_vector_type(2)));
typedef __bf16 bf16x2 __attribute__((ext_vector_type(2)));

#define RZ_BLOCK 256
#define RZ_VPT 2
#define RZ_TILE_BYTES (RZ_BLOCK * RZ_VPT * 16)

__device__ __forceinline__ unsigned int rezero_pair(unsigned int xw, unsigned int fw, float a) {
    f32x2 f;
    f.x = __uint_as_float(fw << 16);
    f.y = __uint_as_float(fw & 0xffff0000u);
    f32x2 p = f * a;
    bf16x2 pb = __builtin_convertvector(p, bf16x2);
    unsigned int pw = __builtin_bit_cast(unsigned int, pb);
    f32x2 t;
    t.x = __uint_as_float(pw << 16);
    t.y = __uint_as_float(pw & 0xffff0000u);
    f32x2 xv;
    xv.x = __uint_as_float(xw << 16);
    xv.y = __uint_as_float(xw & 0xffff0000u);
    f32x2 s = xv + t;
    bf16x2 sb = __builtin_convertvector(s, bf16x2);
    return __builtin_bit_cast(unsigned int, sb);
}

__device__ __forceinline__ u32x4 rezero_vec(u32x4 xv, u32x4 fv, float a) {
    u32x4 o;
    o.x = rezero_pair(xv.x, fv.x, a);
    o.y = rezero_pair(xv.y, fv.y, a);
    o.z = rezero_pair(xv.z, fv.z, a);
    o.w = rezero_pair(xv.w, fv.w, a);
    return o;
}

__global__ __launch_bounds__(RZ_BLOCK) void rezero_kernel(const unsigned short* __restrict__ x,
                                                          const unsigned short* __restrict__ fx,
                                                          const unsigned int* __restrict__ alpha,
                                                          unsigned short* __restrict__ out,
                                                          long long vec_bytes, long long n) {
    const long long base = (long long)blockIdx.x * RZ_TILE_BYTES;
    const long long left = vec_bytes - base;
    const unsigned int nrec = left >= RZ_TILE_BYTES ? (unsigned int)RZ_TILE_BYTES
                                                    : (left > 0 ? (unsigned int)left : 0u);
    __amdgpu_buffer_rsrc_t rx = __builtin_amdgcn_make_buffer_rsrc((char*)x + base, 0, nrec, 0x00020000);
    __amdgpu_buffer_rsrc_t rf = __builtin_amdgcn_make_buffer_rsrc((char*)fx + base, 0, nrec, 0x00020000);
    __amdgpu_buffer_rsrc_t ro = __builtin_amdgcn_make_buffer_rsrc((char*)out + base, 0, nrec, 0x00020000);
    const unsigned int off = threadIdx.x * 16u;
    u32x4 xv[RZ_VPT], fv[RZ_VPT];
#pragma unroll
    for (int j = 0; j < RZ_VPT; ++j) {
        fv[j] = __builtin_amdgcn_raw_buffer_load_b128(rf, off + j * (RZ_BLOCK * 16u), 0,   2);
        xv[j] = __builtin_amdgcn_raw_buffer_load_b128(rx, off + j * (RZ_BLOCK * 16u), 0,   2);
    }
    const float a = __uint_as_float(alpha[0] << 16);
#pragma unroll
    for (int j = 0; j < RZ_VPT; ++j) {
        u32x4 o = rezero_vec(xv[j], fv[j], a);
        __builtin_amdgcn_raw_buffer_store_b128(o, ro, off + j * (RZ_BLOCK * 16u), 0,   17);
    }
    const long long tail0 = vec_bytes >> 1;
    if (tail0 < n && blockIdx.x == 0) {
        const long long i = tail0 + threadIdx.x;
        if (i < n) out[i] = (unsigned short)(rezero_pair((unsigned int)x[i], (unsigned int)fx[i], a) & 0xffffu);
    }
}

__global__ __launch_bounds__(256) void rezero_scalar_kernel(const unsigned short* __restrict__ x,
                                                            const unsigned short* __restrict__ fx,
                                                            const unsigned short* __restrict__ alpha,
                                                            unsigned short* __restrict__ out, long long n,
                                                            long long stride) {
    const float a = __uint_as_float(((unsigned int)alpha[0]) << 16);
    for (long long i = (long long)blockIdx.x * 256 + threadIdx.x; i < n; i += stride)
        out[i] = (unsigned short)(rezero_pair((unsigned int)x[i], (unsigned int)fx[i], a) & 0xffffu);
}

extern "C" void kernel_launch(void* const* d_in, const int* in_sizes, int n_in,
                              void* d_out, int out_size, void* d_ws, size_t ws_size,
                              hipStream_t stream) {
    const unsigned short* x = (const unsigned short*)d_in[0];
    const unsigned short* fx = (const unsigned short*)d_in[1];
    const unsigned short* alpha = (const unsigned short*)d_in[2];
    unsigned short* out = (unsigned short*)d_out;
    const long long n = out_size;
    if (n <= 0) return;
    const bool aligned = ((((uintptr_t)x) | ((uintptr_t)fx) | ((uintptr_t)out)) & 15) == 0 &&
                         (((uintptr_t)alpha) & 3) == 0;
    if (aligned) {
        const long long vec_bytes = (n / 8) * 16;
        long long grid = (vec_bytes + RZ_TILE_BYTES - 1) / RZ_TILE_BYTES;
        if (grid < 1) grid = 1;
        rezero_kernel<<<(unsigned int)grid, RZ_BLOCK, 0, stream>>>(x, fx, (const unsigned int*)alpha, out,
                                                                   vec_bytes, n);
    } else {
        long long grid = (n + 255) / 256;
        if (grid > 4096) grid = 4096;
        rezero_scalar_kernel<<<(unsigned int)grid, 256, 0, stream>>>(x, fx, alpha, out, n, grid * 256);
    }
}
```
